```python
import torch

def _softmax(x, axis=-1):
    return torch.softmax(x, dim=axis)
def _gelu(x):
    return torch.nn.functional.gelu(x, approximate="tanh")
def _silu(x):
    return torch.nn.functional.silu(x)
def _sigmoid(x):
    return torch.sigmoid(x)
def _layernorm(x, axis=-1, eps=1e-5):
    m = x.mean(dim=axis, keepdim=True)
    v = x.var(dim=axis, keepdim=True, unbiased=False)
    return (x - m) / torch.sqrt(v + eps)
def _scatter_add(tgt, idx, src, axis=0):
    return tgt.clone().index_add_(axis, idx, src)
def _segment_sum(src, seg, n):
    out = torch.zeros((n,) + src.shape[1:], dtype=src.dtype)
    return out.index_add_(0, seg, src)
def _pad(x, pw):
    flat = tuple(v for pair in reversed(pw) for v in pair)
    return torch.nn.functional.pad(x, flat)

kernel_name = 'sequential_preLN__linear_kernel_hedgehog__glu_gated_swiglu__prompt'

def setup_inputs():

    _w = (lambda a: (lambda t: t.to(torch.bfloat16) if t.is_floating_point() else t)(torch.as_tensor(a)))
    def _A_setup():
        B, S, D, H = 1, 2048, 576, 32
        Dh = D // H
        return {
            "x": _w((lambda *s: torch.randn(*s, dtype=torch.bfloat16) * 0.05)(B, S, D)),
            "wqkv": _w((lambda *s: torch.randn(*s, dtype=torch.bfloat16) * 0.2)(D, 3 * D)),
            "w_phi": _w((lambda *s: torch.randn(*s, dtype=torch.bfloat16) * 0.2)(H, Dh, Dh) * (Dh ** -0.5)),
            "wo": _w((lambda *s: torch.randn(*s, dtype=torch.bfloat16) * 0.05)(D, D)),
        }
    _A_in = _A_setup()

    def _B_setup():
        B, S, D, F = 1, 2048, 576, 4608
        return {
            "x": _w((lambda *s: torch.randn(*s, dtype=torch.bfloat16) * 0.05)(B, S, D)),
            "w1": _w((lambda *s: torch.randn(*s, dtype=torch.bfloat16) * 0.4)(D, F) * (2.0 / D) ** 0.5),
            "w3": _w((lambda *s: torch.randn(*s, dtype=torch.bfloat16) * 0.4)(D, F) * (2.0 / D) ** 0.5),
            "w2": _w((lambda *s: torch.randn(*s, dtype=torch.bfloat16) * 0.4)(F, D) * (2.0 / F) ** 0.5),
        }
    _B_in = _B_setup()

    return {
        "A_x": _A_in["x"],
        "A_wqkv": _A_in["wqkv"],
        "A_w_phi": _A_in["w_phi"],
        "A_wo": _A_in["wo"],
        "B_x": _B_in["x"],
        "B_w1": _B_in["w1"],
        "B_w3": _B_in["w3"],
        "B_w2": _B_in["w2"],
    }

def reference(A_x, A_wqkv, A_w_phi, A_wo, B_x, B_w1, B_w3, B_w2):
    def _A_ref(x, wqkv, w_phi, wo):
        B, S, D = x.shape
        H = 32; Dh = D // H
        qkv = (lambda x, *d: x.permute(*d))((x @ wqkv).reshape(B, S, 3, H, Dh), 2, 0, 3, 1, 4)
        q, k, v = qkv[0], qkv[1], qkv[2]
        pq = torch.einsum("bhsd,hdf->bhsf", q, w_phi)
        pk = torch.einsum("bhsd,hdf->bhsf", k, w_phi)
        phi_q = torch.concatenate([_softmax(pq, axis=-1), _softmax(-pq, axis=-1)], axis=-1)
        phi_k = torch.concatenate([_softmax(pk, axis=-1), _softmax(-pk, axis=-1)], axis=-1)
        kv = torch.einsum("bhsf,bhsd->bhfd", phi_k, v)
        z  = torch.einsum("bhsf->bhf", phi_k)
        num = torch.einsum("bhqf,bhfd->bhqd", phi_q, kv)
        den = torch.einsum("bhqf,bhf->bhq", phi_q, z)[..., None] + 1e-6
        o = num / den
        return (lambda x, *d: x.permute(*d))(o, 0, 2, 1, 3).reshape(B, S, D) @ wo

    def _B_ref(x, w1, w3, w2):
        gate = torch.einsum("bsd,df->bsf", x, w1)
        up = torch.einsum("bsd,df->bsf", x, w3)
        h = _silu(gate) * up
        out = torch.einsum("bsf,fd->bsd", h, w2)
        return out

    x = A_x
    h = x + _A_ref(_layernorm(x, axis=-1), A_wqkv, A_w_phi, A_wo)
    h2 = h + _B_ref(_layernorm(h, axis=-1), B_w1, B_w3, B_w2)
    return h2
```

```cpp
#include <hip/hip_runtime.h>
#include <hip/hip_bf16.h>
typedef unsigned short u16;
__device__ __forceinline__ float bf2f(u16 v){ return __uint_as_float(((unsigned)v)<<16); }
__device__ __forceinline__ u16 f2bf(float f){ unsigned u=__float_as_uint(f); u += 0x7fffu + ((u>>16)&1u); return (u16)(u>>16); }
constexpr int S=2048, D=576, H=32, DH=18, F=4608;

__global__ void ln_k(const u16* x, u16* y){
  int r=blockIdx.x; int t=threadIdx.x;
  float v[9]; float s=0;
  for(int i=0;i<9;i++){ v[i]=bf2f(x[r*D+t+64*i]); s+=v[i]; }
  for(int o=32;o;o>>=1) s+=__shfl_xor(s,o);
  float m=s/D; float q=0;
  for(int i=0;i<9;i++){ float d=v[i]-m; q+=d*d; }
  for(int o=32;o;o>>=1) q+=__shfl_xor(q,o);
  float rs=1.f/sqrtf(q/D+1e-5f);
  for(int i=0;i<9;i++) y[r*D+t+64*i]=f2bf((v[i]-m)*rs);
}
__global__ void gemm_k(const u16* A, const u16* B, u16* C, const u16* res, int M,int N,int K,int mode){
  __shared__ float As[16][65]; __shared__ float Bs[16][65];
  int tx=threadIdx.x&15, ty=threadIdx.x>>4; int bm=blockIdx.y*64, bn=blockIdx.x*64;
  float acc[4][4]={};
  for(int k0=0;k0<K;k0+=16){
    for(int i=threadIdx.x;i<1024;i+=256){ int r=i>>4,c=i&15; As[c][r]=bf2f(A[(size_t)(bm+r)*K+k0+c]); }
    for(int i=threadIdx.x;i<1024;i+=256){ int r=i>>6,c=i&63; Bs[r][c]=(bn+c<N)?bf2f(B[(size_t)(k0+r)*N+bn+c]):0.f; }
    __syncthreads();
    for(int k=0;k<16;k++){ float a[4],b[4]; for(int i=0;i<4;i++){a[i]=As[k][ty*4+i]; b[i]=Bs[k][tx*4+i];}
      for(int i=0;i<4;i++)for(int j=0;j<4;j++) acc[i][j]+=a[i]*b[j]; }
    __syncthreads();
  }
  for(int i=0;i<4;i++)for(int j=0;j<4;j++){ int r=bm+ty*4+i,c=bn+tx*4+j; if(c<N){ float v=acc[i][j]; if(mode==1) v=bf2f(f2bf(v))+bf2f(res[(size_t)r*N+c]); C[(size_t)r*N+c]=f2bf(v);} }
}
__global__ __launch_bounds__(256) void phi_k(const u16* qkv, const u16* wphi, float* phi){
  int idx=blockIdx.x*blockDim.x+threadIdx.x;
  if(idx>=S*H*2) return;
  int which=idx/(S*H); int rem=idx%(S*H); int s=rem/H,h=rem%H;
  float q[DH]; _Pragma("unroll") for(int d=0;d<DH;d++) q[d]=bf2f(qkv[(size_t)s*1728+which*576+h*DH+d]);
  float p[DH]; float mx=-1e30f, mn=1e30f;
  _Pragma("unroll") for(int f=0;f<DH;f++){ float a=0; _Pragma("unroll") for(int d=0;d<DH;d++) a+=q[d]*bf2f(wphi[(h*DH+d)*DH+f]); p[f]=a; mx=fmaxf(mx,a); mn=fminf(mn,a);}
  float s1=0,s2=0; float e1[DH],e2[DH];
  _Pragma("unroll") for(int f=0;f<DH;f++){ e1[f]=__expf(p[f]-mx); s1+=e1[f]; e2[f]=__expf(mn-p[f]); s2+=e2[f]; }
  float* o=phi+(size_t)idx*36;
  _Pragma("unroll") for(int f=0;f<DH;f++){ o[f]=e1[f]/s1; o[18+f]=e2[f]/s2; }
}
__global__ void kv_k(const u16* qkv, const float* phi, float* kv){
  int h=blockIdx.x; int t=threadIdx.x;
  if(t>=36*19) return;
  int f=t/19,d=t%19; float a=0;
  const float* pk=phi+(size_t)S*H*36;
  for(int s=0;s<S;s++){ float p=pk[((size_t)s*H+h)*36+f]; float v=(d<18)?bf2f(qkv[(size_t)s*1728+1152+h*DH+d]):1.f; a+=p*v; }
  kv[(h*36+f)*19+d]=a;
}
__global__ void o_k(const float* phi, const float* kv, u16* o){
  int idx=blockIdx.x*blockDim.x+threadIdx.x; if(idx>=S*H) return; int s=idx/H,h=idx%H;
  const float* pq=phi+(size_t)idx*36; float num[19]; for(int d=0;d<19;d++) num[d]=0;
  for(int f=0;f<36;f++){ float p=pq[f]; for(int d=0;d<19;d++) num[d]+=p*kv[(h*36+f)*19+d]; }
  float den=num[18]+1e-6f;
  for(int d=0;d<18;d++) o[(size_t)s*D+h*DH+d]=f2bf(num[d]/den);
}
__global__ void swiglu_k(const u16* g,const u16* u,u16* o,int n){ int i=blockIdx.x*blockDim.x+threadIdx.x; if(i<n){ float a=bf2f(g[i]); float s=a/(1.f+__expf(-a)); o[i]=f2bf(s*bf2f(u[i])); } }

extern "C" void kernel_launch(void* const* d_in, const int* in_sizes, int n_in, void* d_out, int out_size, void* d_ws, size_t ws_size, hipStream_t stream) {
  const u16* x=(const u16*)d_in[0]; const u16* wqkv=(const u16*)d_in[1]; const u16* wphi=(const u16*)d_in[2]; const u16* wo=(const u16*)d_in[3];
  const u16* w1=(const u16*)d_in[5]; const u16* w3=(const u16*)d_in[6]; const u16* w2=(const u16*)d_in[7];
  char* ws=(char*)d_ws; size_t off=0; auto al=[&](size_t b){ void* p=ws+off; off+=(b+255)&~(size_t)255; return p; };
  u16* xn=(u16*)al(S*D*2); u16* qkv=(u16*)al((size_t)S*1728*2); float* phi=(float*)al((size_t)2*S*H*36*4); float* kv=(float*)al(H*36*19*4);
  u16* o=(u16*)al(S*D*2); u16* h=(u16*)al(S*D*2); u16* hn=(u16*)al(S*D*2); u16* g=(u16*)al((size_t)S*F*2); u16* u=(u16*)al((size_t)S*F*2); u16* hm=(u16*)al((size_t)S*F*2);
  ln_k<<<S,64,0,stream>>>(x,xn);
  gemm_k<<<dim3(27,32),256,0,stream>>>(xn,wqkv,qkv,nullptr,S,1728,D,0);
  phi_k<<<S*H*2/256,256,0,stream>>>(qkv,wphi,phi);
  kv_k<<<H,704,0,stream>>>(qkv,phi,kv);
  o_k<<<S*H/256,256,0,stream>>>(phi,kv,o);
  gemm_k<<<dim3(9,32),256,0,stream>>>(o,wo,h,x,S,D,D,1);
  ln_k<<<S,64,0,stream>>>(h,hn);
  gemm_k<<<dim3(72,32),256,0,stream>>>(hn,w1,g,nullptr,S,F,D,0);
  gemm_k<<<dim3(72,32),256,0,stream>>>(hn,w3,u,nullptr,S,F,D,0);
  swiglu_k<<<S*F/256,256,0,stream>>>(g,u,hm,S*F);
  gemm_k<<<dim3(9,32),256,0,stream>>>(hm,w2,(u16*)d_out,h,S,D,F,1);
}
```

```cpp
constexpr int K4_SLOT=32768+18432, K4_LDS=3*K4_SLOT;
DI void phase4(const Params& p, LAS unsigned char* lds){
  const int tid=threadIdx.x, lane=tid&63, wid=__builtin_amdgcn_readfirstlane(tid>>6);
  const int wm=wid&3, wn=wid>>2;
  const int bid=blockIdx.x, sl_=bid&7, idx=bid>>3;
  const int ct=idx&3, mt=idx>>2;
  const int g=lane>>4, q=(lane>>2)&3, pp=lane&3;
  const unsigned ldsb=(unsigned)(size_t)lds;
  const unsigned voffA=lane*16;
  const char* aBase=p.ws+WS_HM+((size_t)(mt*16+wid*2)*144+18*sl_)*1024;
  auto wrow=[](unsigned kb9, unsigned rho)->unsigned{ const unsigned j=kb9/9u, r=kb9%9u;
    return r<8u ? 144u*(2u*j+(r>>2))+32u*(r&3u)+rho : 144u*(2u*j+((rho>>3)&1u))+128u+8u*((rho>>2)&1u)+4u*(rho>>4)+(rho&3u); };
  const char* wBase=(const char*)p.w2+((size_t)sl_*576*576+ct*144)*2;
  const unsigned o0=wid*1024+lane*16, o1=o0+8192, o2=16384+(wid&1)*1024+lane*16;
  const unsigned ir0=o0/288, ic0=o0%288, ir1=o1/288, ic1=o1%288, ir2=o2/288, ic2=o2%288;
  auto issue=[&](int st){
    const unsigned sl=ldsb+(st%3)*K4_SLOT;
    glds16(wrow(2*st+(ir0>>5),ir0&31)*1152+ic0,wBase,sl+32768+wid*1024);
    glds16(wrow(2*st+(ir1>>5),ir1&31)*1152+ic1,wBase,sl+32768+8192+wid*1024);
    if(wid<2) glds16(wrow(2*st+(ir2>>5),ir2&31)*1152+ic2,wBase,sl+32768+16384+wid*1024);
    const char* ab=aBase+(size_t)(2*st)*1024;
    glds16(voffA,ab,sl+(wid*4)*1024); glds16(voffA,ab+1024,sl+(wid*4+1)*1024);
    glds16(voffA,ab+144*1024,sl+(wid*4+2)*1024); glds16(voffA,ab+145*1024,sl+(wid*4+3)*1024);
  };
  issue(0); BAR(); issue(1);
  const unsigned aRd=ldsb+(8*wm)*1024+(lane&15)*64+((g^((lane>>2)&2))*16);
  const unsigned bRd=ldsb+32768+(4*g+q)*288+(wn?160:0)+pp*8;
  f32x4 acc[4][5];
#pragma unroll
  for(int mi=0;mi<4;mi++)
#pragma unroll
    for(int t=0;t<5;t++) acc[mi][t]=(f32x4){0,0,0,0};
  WAIT_V(6); BAR();
  if(wn==1) BAR();
  for(int st=0;st<9;st++){
    const unsigned so=(st%3)*K4_SLOT;
    bf16x8 xa[2][4], wf[2][5];
#pragma unroll
    for(int kk=0;kk<2;kk++){
#pragma unroll
      for(int mi=0;mi<4;mi++) xa[kk][mi]=ld_frag(aRd+so+(mi*2+kk)*1024);
#pragma unroll
      for(int t=0;t<4;t++) wf[kk][t]=tr_frag(bRd+so+(32*kk)*288+t*32,bRd+so+(32*kk+16)*288+t*32);
      if(wn==0) wf[kk][4]=tr_frag(bRd+so+(32*kk)*288+128,bRd+so+(32*kk+16)*288+128);
    }
    __builtin_amdgcn_sched_barrier(0);
    if(st<7) issue(st+2);
    if(st<7){ WAIT_V(6); } else { WAIT_V(0); }
    WAIT_L(0); BAR();
    __builtin_amdgcn_s_setprio(1);
#pragma unroll
    for(int kk=0;kk<2;kk++){
#pragma unroll
      for(int mi=0;mi<4;mi++){
#pragma unroll
        for(int t=0;t<4;t++) acc[mi][t]=MFMA16(wf[kk][t],xa[kk][mi],acc[mi][t]);
      }
      if(wn==0){
#pragma unroll
        for(int mi=0;mi<4;mi++) acc[mi][4]=MFMA16(wf[kk][4],xa[kk][mi],acc[mi][4]);
      }
    }
    __builtin_amdgcn_s_setprio(0);
    BAR();
  }
  if(wn==0) BAR();
#pragma unroll
  for(int mi=0;mi<4;mi++){
    char* bp=p.ws+WS_PART+(((size_t)sl_*128+mt*16+4*wm+mi)*4+ct)*4608+(wn?2560:0);
#pragma unroll
    for(int tp=0;tp<2;tp++){ const f32x4 a=acc[mi][2*tp], b=acc[mi][2*tp+1];
      *(u32x4*)(bp+tp*1024+lane*16)=(u32x4){pk_bf16(a[0],a[1]),pk_bf16(a[2],a[3]),pk_bf16(b[0],b[1]),pk_bf16(b[2],b[3])}; }
    if(wn==0){ const f32x4 a=acc[mi][4]; *(u32x2*)(bp+2048+lane*8)=(u32x2){pk_bf16(a[0],a[1]),pk_bf16(a[2],a[3])}; }
  }
}
__global__ __launch_bounds__(512,2) void k4_kernel(Params p){ extern __shared__ __attribute__((aligned(16))) unsigned char shm[]; phase4(p,(LAS unsigned char*)shm); }

DI void p5_chunk(const Params& p, int unit, int ch){
  const int mb=unit>>2, ct=unit&3;
  const char* src=p.ws+WS_PART+(size_t)unit*4608+ch*16;
  u32x4 v[8];
#pragma unroll
  for(int s=0;s<8;s++) v[s]=*(const u32x4*)(src+(size_t)s*128*4*4608);
  const int bo=ch*16; int tA,tB,lnA,lnB;
  if(bo<2048){ tA=2*(bo>>10); tB=tA+1; lnA=lnB=(bo&1023)>>4; }
  else if(bo<2560){ tA=tB=4; lnA=(bo-2048)>>3; lnB=lnA+1; }
  else { const int b2=bo-2560; tA=5+2*(b2>>10); tB=tA+1; lnA=lnB=(b2&1023)>>4; }
  const int rowA=mb*16+(lnA&15), colA=ct*144+16*tA+4*(lnA>>4), rowB=mb*16+(lnB&15), colB=ct*144+16*tB+4*(lnB>>4);
  const u16* h=(const u16*)(p.ws+WS_H);
  const u32x2 hA=*(const u32x2*)(h+(size_t)rowA*576+colA), hB=*(const u32x2*)(h+(size_t)rowB*576+colB);
  float s[8]={0,0,0,0,0,0,0,0};
#pragma unroll
  for(int i=0;i<8;i++){
#pragma unroll
    for(int j=0;j<4;j++){ s[2*j]+=bflo(v[i][j]); s[2*j+1]+=bfhi(v[i][j]); } }
  const u32x2 oA={pk_bf16(bflo(hA[0])+rbf(s[0]),bfhi(hA[0])+rbf(s[1])),pk_bf16(bflo(hA[1])+rbf(s[2]),bfhi(hA[1])+rbf(s[3]))};
  const u32x2 oB={pk_bf16(bflo(hB[0])+rbf(s[4]),bfhi(hB[0])+rbf(s[5])),pk_bf16(bflo(hB[1])+rbf(s[6]),bfhi(hB[1])+rbf(s[7]))};
  *(u32x2*)(p.out+(size_t)rowA*576+colA)=oA; *(u32x2*)(p.out+(size_t)rowB*576+colB)=oB;
}
DI void phase5(const Params& p){
  const int tid=threadIdx.x, bid=blockIdx.x;
  { const int c=tid; p5_chunk(p,2*bid+(c>=288),c>=288?c-288:c); }
  if(tid<64){ const int c=512+tid; p5_chunk(p,2*bid+1,c-288); }
}
__global__ __launch_bounds__(512,2) void k5_kernel(Params p){ phase5(p); }

extern "C" void kernel_launch(void* const* d_in, const int* in_sizes, int n_in, void* d_out, int out_size, void* d_ws, size_t ws_size, hipStream_t stream) {
  Params p; p.x=(const u16*)d_in[0]; p.wqkv=(const u16*)d_in[1]; p.wphi=(const u16*)d_in[2]; p.wo=(const u16*)d_in[3];
  p.w1=(const u16*)d_in[5]; p.w3=(const u16*)d_in[6]; p.w2=(const u16*)d_in[7]; p.out=(u16*)d_out; p.ws=(char*)d_ws;
  hipFuncSetAttribute((const void*)k1_kernel,hipFuncAttributeMaxDynamicSharedMemorySize,P1_LDS);
  hipFuncSetAttribute((const void*)k2b_kernel,hipFuncAttributeMaxDynamicSharedMemorySize,P2B_LDS);
  hipFuncSetAttribute((const void*)k3_kernel,hipFuncAttributeMaxDynamicSharedMemorySize,K3_LDS);
  hipFuncSetAttribute((const void*)k4_kernel,hipFuncAttributeMaxDynamicSharedMemorySize,K4_LDS);
  k1_kernel<<<256,512,P1_LDS,stream>>>(p);
  k2a_kernel<<<256,512,P2A_LDS,stream>>>(p);
  k2b_kernel<<<256,512,P2B_LDS,stream>>>(p);
  k3_kernel<<<256,512,K3_LDS,stream>>>(p);
  k4_kernel<<<256,512,K4_LDS,stream>>>(p);
  k5_kernel<<<256,512,0,stream>>>(p);
}
```

```cpp
#include <hip/hip_runtime.h>
#include <hip/hip_bf16.h>
#include <type_traits>
typedef unsigned short u16;
typedef unsigned long long u64;
typedef short s16x4 __attribute__((ext_vector_type(4)));
typedef short s16x8 __attribute__((ext_vector_type(8)));
typedef __bf16 bf16x8 __attribute__((ext_vector_type(8)));
typedef __bf16 bf16x2 __attribute__((ext_vector_type(2)));
typedef float f32x4 __attribute__((ext_vector_type(4)));
typedef float f32x2 __attribute__((ext_vector_type(2)));
typedef unsigned u32x4 __attribute__((ext_vector_type(4)));
typedef unsigned u32x2 __attribute__((ext_vector_type(2)));
typedef __amdgpu_buffer_rsrc_t rsrc_t;
#define LAS __attribute__((address_space(3)))
#define GAS __attribute__((address_space(1)))
#define DI __device__ __forceinline__
DI float bf2f(u16 v){ return __uint_as_float(((unsigned)v)<<16); }
DI float bflo(unsigned v){ return __uint_as_float(v<<16); }
DI float bfhi(unsigned v){ return __uint_as_float(v&0xffff0000u); }
DI unsigned pk_bf16(float lo, float hi){ const f32x2 v={lo,hi}; return __builtin_bit_cast(unsigned,__builtin_convertvector(v,bf16x2)); }
DI float rbf(float x){ return bflo(pk_bf16(x,x)); }
constexpr int S=2048, D=576, H=32, DH=18, F=4608;
constexpr float LOG2E=1.4426950408889634f;
#define WAIT_V(n) asm volatile("s_waitcnt vmcnt(" #n ")" ::: "memory")
#define WAIT_L(n) asm volatile("s_waitcnt lgkmcnt(" #n ")" ::: "memory")
#define BAR() do{ asm volatile("" ::: "memory"); __builtin_amdgcn_s_barrier(); asm volatile("" ::: "memory"); }while(0)
#define LBAR() do{ asm volatile("s_waitcnt lgkmcnt(0)" ::: "memory"); __builtin_amdgcn_s_barrier(); asm volatile("" ::: "memory"); }while(0)
#define SB0() __builtin_amdgcn_sched_barrier(0)
DI void glds16(unsigned voff, const void* sbase, unsigned ldsaddr){
  asm volatile("s_mov_b32 m0, %2\n\ts_nop 0\n\tglobal_load_lds_dwordx4 %0, %1" :: "v"(voff), "s"(sbase), "s"(ldsaddr) : "memory"); }
DI void glds16c(unsigned voff, const void* sbase, unsigned ldsaddr){
  asm volatile("s_mov_b32 m0, %2\n\ts_nop 0\n\tglobal_load_lds_dwordx4 %0, %1 sc1" :: "v"(voff), "s"(sbase), "s"(ldsaddr) : "memory"); }
DI void glds16_lo8(unsigned voff, const void* sbase, unsigned ldsaddr){
  asm volatile("s_mov_b32 m0, %2\n\ts_mov_b64 exec, 0xff\n\tglobal_load_lds_dwordx4 %0, %1\n\ts_mov_b64 exec, -1" :: "v"(voff), "s"(sbase), "s"(ldsaddr) : "memory"); }
DI s16x4 tr4(unsigned a){ return __builtin_amdgcn_ds_read_tr16_b64_v4i16((LAS s16x4*)(size_t)a); }
DI bf16x8 cat8(s16x4 a, s16x4 b){ return __builtin_bit_cast(bf16x8,__builtin_shufflevector(a,b,0,1,2,3,4,5,6,7)); }
DI bf16x8 tr_frag(unsigned a0, unsigned a1){ return cat8(tr4(a0),tr4(a1)); }
DI bf16x8 ld_frag(unsigned a){ return *(LAS bf16x8*)(size_t)a; }
#define MFMA16(a,b,c) __builtin_amdgcn_mfma_f32_16x16x32_bf16((a),(b),(c),0,0,0)
#define MFMA16K16(a,b,c) __builtin_amdgcn_mfma_f32_16x16x16bf16_1k((a),(b),(c),0,0,0)
DI float dot2(unsigned a, unsigned b, float c){ return __builtin_amdgcn_fdot2_f32_bf16(__builtin_bit_cast(bf16x2,a),__builtin_bit_cast(bf16x2,b),c,false); }
DI float xr_max(float x){ auto s=__builtin_amdgcn_permlane16_swap(__float_as_uint(x),__float_as_uint(x),false,false); x=fmaxf(__uint_as_float(s[0]),__uint_as_float(s[1]));
  auto t=__builtin_amdgcn_permlane32_swap(__float_as_uint(x),__float_as_uint(x),false,false); return fmaxf(__uint_as_float(t[0]),__uint_as_float(t[1])); }
DI float xr_min(float x){ auto s=__builtin_amdgcn_permlane16_swap(__float_as_uint(x),__float_as_uint(x),false,false); x=fminf(__uint_as_float(s[0]),__uint_as_float(s[1]));
  auto t=__builtin_amdgcn_permlane32_swap(__float_as_uint(x),__float_as_uint(x),false,false); return fminf(__uint_as_float(t[0]),__uint_as_float(t[1])); }
DI float xr_sum(float x){ auto s=__builtin_amdgcn_permlane16_swap(__float_as_uint(x),__float_as_uint(x),false,false); x=__uint_as_float(s[0])+__uint_as_float(s[1]);
  auto t=__builtin_amdgcn_permlane32_swap(__float_as_uint(x),__float_as_uint(x),false,false); return __uint_as_float(t[0])+__uint_as_float(t[1]); }
constexpr unsigned WS_FLG  = 0;
constexpr unsigned FL_EPOCH=0, FL_TMO=128, FL_1=1024  , FL_2=FL_1+2048  , FL_3=FL_2+2048  , FL_4=FL_3+2048  , FL_5=FL_4+8192  ;
constexpr unsigned WS_HM   = 32768;
constexpr unsigned WS_PART = WS_HM + 128u*144*1024;
constexpr unsigned WS_KVP  = WS_PART + 8u*128*4*4608;
constexpr unsigned WS_O    = WS_KVP + 256u*4096;
constexpr unsigned WS_HC   = WS_O + 128u*18*1024;
constexpr unsigned WS_H    = WS_HC + 128u*18*1024;
constexpr unsigned WS_SSQ  = WS_H + S*D*2;
constexpr unsigned WS_MEANX= WS_SSQ + S*16*4;
constexpr unsigned WS_WMF  = WS_MEANX + S*4;
constexpr unsigned WS_END  = WS_WMF + 2048;
struct Params { const u16* x; const u16* wqkv; const u16* wphi; const u16* wo; const u16* w1; const u16* w3; const u16* w2; u16* out; char* ws; };
DI void st16(rsrc_t r, unsigned off, u32x4 v){ __builtin_amdgcn_raw_buffer_store_b128(v,r,off,0,16); }
DI void st8(rsrc_t r, unsigned off, u32x2 v){ __builtin_amdgcn_raw_buffer_store_b64(v,r,off,0,16); }
DI void st4(rsrc_t r, unsigned off, unsigned v){ __builtin_amdgcn_raw_buffer_store_b32(v,r,off,0,16); }
DI void st2(rsrc_t r, unsigned off, u16 v){ __builtin_amdgcn_raw_buffer_store_b16(v,r,off,0,16); }
DI u32x4 ld16c(rsrc_t r, unsigned off){ return __builtin_bit_cast(u32x4,__builtin_amdgcn_raw_buffer_load_b128(r,off,0,16)); }
DI u32x2 ld8c(rsrc_t r, unsigned off){ return __builtin_bit_cast(u32x2,__builtin_amdgcn_raw_buffer_load_b64(r,off,0,16)); }
DI unsigned ld4c(rsrc_t r, unsigned off){ return __builtin_amdgcn_raw_buffer_load_b32(r,off,0,16); }
DI u64 flag_ld(const char* ws, unsigned off){ return __hip_atomic_load((GAS u64*)(ws+off),__ATOMIC_RELAXED,__HIP_MEMORY_SCOPE_AGENT); }
DI void flag_st(char* ws, unsigned off, u64 v){ __hip_atomic_store((GAS u64*)(ws+off),v,__ATOMIC_RELAXED,__HIP_MEMORY_SCOPE_AGENT); }
#define STK(i)
DI int otid(){ int t=threadIdx.x; asm volatile("" : "+v"(t)); return t; }
DI void publish(char* ws, unsigned off, u64 tag, int si=0){ WAIT_V(0); STK(32+si*4); BAR(); if(otid()==0) flag_st(ws,off,tag); STK(33+si*4); }
DI void poll_flags(char* ws, unsigned off, int n, u64 tag){
  const int lane=otid()&63; unsigned spins=0;
  for(;;){
    const u64 v=(lane<n)?flag_ld(ws,off+lane*8):tag;
    if(__all(v==tag)) break;
    __builtin_amdgcn_s_sleep(1);
    ++spins;
    if(spins>(1u<<16)){ if(lane==0) flag_st(ws,FL_TMO,tag); break; }
    if((spins&255u)==0u && flag_ld(ws,FL_TMO)==tag) break;
  }
  __builtin_amdgcn_fence(__ATOMIC_ACQUIRE,"wavefront");
}
DI void wait_flags(char* ws, unsigned off, int n, u64 tag, int si=0){
  if(otid()<64){ poll_flags(ws,off,n,tag); STK(34+si*4); STK(35+si*4); }
  BAR();
}

constexpr int P1_SLOT=34816, P1_WF=4*P1_SLOT, P1_RAW=P1_WF+13*512, P1_CS=P1_RAW+2048, P1_CSF=P1_CS+8*448, P1_WO=P1_CSF+448;
constexpr int P1_F=0, P1_V=128*160;
struct PhiQ { u32x4 a, b; u32x2 x; };
struct PreQ { f32x4 a, b; float xa, xb; };
DI void dual_softmax(const f32x4& v, float xe, bool hx, unsigned (&pos)[2], unsigned (&neg)[2], float& xpos, float& xneg){
  float mx=fmaxf(fmaxf(v[0],v[1]),fmaxf(v[2],v[3])), mn=fminf(fminf(v[0],v[1]),fminf(v[2],v[3]));
  mx=hx?fmaxf(mx,xe):mx; mn=hx?fminf(mn,xe):mn;
  mx=xr_max(mx); mn=xr_min(mn);
  const float a=mx*LOG2E, b=mn*LOG2E;
  float ep[4], en[4];
#pragma unroll
  for(int r=0;r<4;r++){ ep[r]=__builtin_amdgcn_exp2f(__builtin_fmaf(v[r],LOG2E,-a)); en[r]=__builtin_amdgcn_exp2f(__builtin_fmaf(-v[r],LOG2E,b)); }
  const float xp=hx?__builtin_amdgcn_exp2f(__builtin_fmaf(xe,LOG2E,-a)):0.f, xn=hx?__builtin_amdgcn_exp2f(__builtin_fmaf(-xe,LOG2E,b)):0.f;
  float sp_=(ep[0]+ep[1])+(ep[2]+ep[3])+xp, sn_=(en[0]+en[1])+(en[2]+en[3])+xn;
  sp_=xr_sum(sp_); sn_=xr_sum(sn_);
  const float rp=__builtin_amdgcn_rcpf(sp_), rn=__builtin_amdgcn_rcpf(sn_);
  pos[0]=pk_bf16(ep[0]*rp,ep[1]*rp); pos[1]=pk_bf16(ep[2]*rp,ep[3]*rp);
  neg[0]=pk_bf16(en[0]*rn,en[1]*rn); neg[1]=pk_bf16(en[2]*rn,en[3]*rn);
  xpos=xp*rp; xneg=xn*rn;
}
DI PhiQ finish_q(const PreQ& pq){
  const int g=(otid()&63)>>4; unsigned pos[2][2], neg[2][2]; float xp[2], xn[2];
  dual_softmax(pq.a,pq.xa,g<2,pos[0],neg[0],xp[0],xn[0]); dual_softmax(pq.b,pq.xb,g<2,pos[1],neg[1],xp[1],xn[1]);
  PhiQ ph; ph.a=(u32x4){pos[0][0],pos[0][1],neg[0][0],neg[0][1]}; ph.b=(u32x4){pos[1][0],pos[1][1],neg[1][0],neg[1][1]}; ph.x=(u32x2){pk_bf16(xp[0],xn[0]),pk_bf16(xp[1],xn[1])};
  asm volatile("" : "+v"(ph.a), "+v"(ph.b), "+v"(ph.x));
  return ph; }
DI PreQ phase1(const Params& p, LAS unsigned char* lds, rsrc_t wsr, int rt, int hp){
  const int tid=otid(), lane=tid&63, wid=__builtin_amdgcn_readfirstlane(tid>>6), bid=blockIdx.x;
  const int g=lane>>4, q=(lane>>2)&3, pp=lane&3, r16=lane&15;
  const unsigned ldsb=(unsigned)(size_t)lds;
  if(wid==7){ const char* wp=(const char*)p.wphi+(size_t)hp*1296; glds16(lane*16,wp,ldsb+P1_RAW); glds16((64+(lane<17?lane:16))*16,wp,ldsb+P1_RAW+1024); }
  if(bid<72){ const char* wr=(const char*)p.wo+(size_t)(8*bid+wid)*1152; glds16(lane*16,wr,ldsb+P1_WO+wid*1152); glds16_lo8(1024+lane*16,wr,ldsb+P1_WO+wid*1152+1024); }
  const unsigned voffX0=(unsigned)((16*wid+(lane>>3))*1152+(((lane&7)^(lane>>4))*16));
  const unsigned voffX1=(unsigned)((16*wid+8+(lane>>3))*1152+(((lane&7)^(4+(lane>>4)))*16));
  const char* xBase=(const char*)p.x+(size_t)rt*128*1152;
  const unsigned cX=ldsb+wid*2048, cW=ldsb+16384+wid*1024;
  glds16(voffX0,xBase,cX); glds16(voffX1,xBase,cX+1024);
  auto wvoff=[&](int id)->unsigned{ const unsigned o=1024u*id+16u*lane; const unsigned row=o/288u, ci0=(o%288u)>>4; const unsigned ci=ci0<15?ci0:0; const unsigned ty=ci/5u, j=ci%5u;
    return row*3456u+(576u*ty+36u*hp+(j<4?8u*j:28u))*2u; };
  const unsigned voffW0=wvoff(wid), voffW1=wvoff(wid+8), voffW2=wvoff(16+(wid&1));
  const char* wBase=(const char*)p.wqkv;
  glds16(voffW0,wBase,cW); glds16(voffW1,wBase,cW+8192); if(wid<2) glds16(voffW2,wBase,cW+16384);
  BAR();
#pragma unroll
  for(int s=1;s<3;s++){ const unsigned sl=s*P1_SLOT; const char* wb=wBase+(size_t)s*64*3456; const char* xb=xBase+s*128;
    glds16(voffW0,wb,sl+cW); glds16(voffW1,wb,sl+cW+8192); if(wid<2) glds16(voffW2,wb,sl+cW+16384);
    glds16(voffX0,xb,sl+cX); glds16(voffX1,xb,sl+cX+1024); }
  WAIT_V(8); BAR();
  STK(1);
  auto side_work=[&](){
  for(int en=tid;en<832;en+=512){
    const int fr=en>>6, l=en&63, i=l&15, gg=l>>4;
    const int T=fr<8?((fr+1)>>1):(fr-8);
    int tout,hout,f; bool valid=true;
    if(fr<8){ const int O=fr>>1; tout=O>>1; hout=O&1; f=i; } else { const int gp=i>>2, r=i&3; tout=r>>1; hout=r&1; f=16+gp; valid=gp<2; }
    const int Q=4*T+gg, tin=Q/9, cb=4*(Q%9);
    unsigned v[4];
#pragma unroll
    for(int j=0;j<4;j++){ const int c=cb+j, hin=c/18, d=c%18; const bool ok=valid&&tin==tout&&hin==hout;
      const unsigned a=ok?(unsigned)(((hin*18+d)*18+f)*2):0u; const unsigned w=*(LAS u16*)(lds+P1_RAW+a); v[j]=ok?w:0u; }
    *(LAS u32x2*)(lds+P1_WF+en*8)=(u32x2){v[0]|(v[1]<<16),v[2]|(v[3]<<16)};
  }
  if(bid<72){
    float s=0.f;
#pragma unroll
    for(int i=0;i<9;i++) s+=bf2f(*(LAS u16*)(lds+P1_WO+wid*1152+(lane+64*i)*2));
#pragma unroll
    for(int o=1;o<16;o<<=1) s+=__shfl_xor(s,o);
    s=xr_sum(s);
    const int R=8*bid+wid, h2=R/36, c=R%36; int ix;
    if(c<32) ix=h2*32+((c>>2)&3)*8+4*(c>>4)+(c&3); else ix=(16+(h2>>3))*32+((h2&7)>>1)*8+4*(h2&1)+(c-32);
    if(lane==0) st2(wsr,WS_WMF+ix*2,(u16)(pk_bf16(s*(1.f/576.f),0.f)&0xffffu)); }
  };
  const unsigned e=(unsigned)((g>>1)^((lane>>1)&7));
  const unsigned xRd=ldsb+(unsigned)((16*wid+r16)*128)+e*16+(g&1)*8;
  unsigned wRd[7];
#pragma unroll
  for(int T=0;T<7;T++){ int Q=4*T+pp; if(Q>26) Q=26; const int ty=Q/9, j=Q%9; wRd[T]=ldsb+(unsigned)(16384+(4*g+q)*288+80*ty+(j<8?8*j:72)); }
  f32x4 acc[7], accs={0,0,0,0}, accx={0,0,0,0}, accq={0,0,0,0};
#pragma unroll
  for(int T=0;T<7;T++) acc[T]=(f32x4){0,0,0,0};
  unsigned wCs; { int Q=4*(wid<7?wid:6)+pp; if(Q>26) Q=26; const int ty=Q/9, j=Q%9; wCs=ldsb+(unsigned)(16384+(4*g+q)*288+80*ty+(j<8?8*j:72)); }
  const unsigned ONES=0x3F803F80u;
  const bf16x8 onesf=__builtin_bit_cast(bf16x8,(u32x4){ONES,ONES,ONES,ONES});
  s16x4 xl0,xh0,xl1,xh1, l0[8],h0[8],l1[8],h1[8];
  unsigned sw=3*P1_SLOT; const char* wb=wBase+(size_t)3*64*3456; const char* xb=xBase+3*128;
  auto dma=[&](int k){
    if(k==0) asm volatile("s_add_u32 m0, %0, %1\n\ts_nop 0\n\tglobal_load_lds_dwordx4 %2, %3" :: "s"(sw),"s"(cW),"v"(voffW0),"s"(wb) : "memory","scc");
    if(k==1) asm volatile("s_add_u32 m0, %0, %1\n\ts_nop 0\n\tglobal_load_lds_dwordx4 %2, %3" :: "s"(sw),"s"(cW+8192),"v"(voffW1),"s"(wb) : "memory","scc");
    if(k==2){ if(wid<2) asm volatile("s_add_u32 m0, %0, %1\n\ts_nop 0\n\tglobal_load_lds_dwordx4 %2, %3" :: "s"(sw),"s"(cW+16384),"v"(voffW2),"s"(wb) : "memory","scc"); }
    if(k==3) asm volatile("s_add_u32 m0, %0, %1\n\ts_nop 0\n\tglobal_load_lds_dwordx4 %2, %3" :: "s"(sw),"s"(cX),"v"(voffX0),"s"(xb) : "memory","scc");
    if(k==4) asm volatile("s_add_u32 m0, %0, %1\n\ts_nop 0\n\tglobal_load_lds_dwordx4 %2, %3" :: "s"(sw),"s"(cX+1024),"v"(voffX1),"s"(xb) : "memory","scc");
  };
  unsigned xa[4]={xRd,xRd^32u,xRd^64u,xRd^96u};
  auto step=[&](s16x4& XL, s16x4& XH, s16x4 (&L)[8], s16x4 (&Hh)[8], s16x4& NL, s16x4& NH, s16x4 (&LN)[8], s16x4 (&HN)[8], int sp, bool rd, bool dm){
    const bf16x8 xf=cat8(XL,XH);
    if(rd){ NL=*(LAS s16x4*)(size_t)(xa[2*sp]); NH=*(LAS s16x4*)(size_t)(xa[2*sp+1]); }
#pragma unroll
    for(int T=0;T<7;T++){
      if(rd){ LN[T]=tr4(wRd[T]+(32*sp)*288); HN[T]=tr4(wRd[T]+(32*sp+16)*288); }
      acc[T]=MFMA16(cat8(L[T],Hh[T]),xf,acc[T]);
    }
    if(rd){ LN[7]=tr4(wCs+(32*sp)*288); HN[7]=tr4(wCs+(32*sp+16)*288); }
    accs=MFMA16(cat8(L[7],Hh[7]),onesf,accs); accx=MFMA16(onesf,xf,accx); accq=MFMA16(xf,xf,accq);
    __builtin_amdgcn_sched_group_barrier(0x100,2,0);
#pragma unroll
    for(int T=0;T<8;T++){ __builtin_amdgcn_sched_group_barrier(0x008,1,0); __builtin_amdgcn_sched_group_barrier(0x100,2,0); }
    __builtin_amdgcn_sched_group_barrier(0x008,2,0);
    SB0();
    if(dm){ dma(0); dma(1); dma(2); dma(3); dma(4); wb+=64*3456; xb+=128; SB0(); }
  };
  { xl0=*(LAS s16x4*)(size_t)(xa[0]); xh0=*(LAS s16x4*)(size_t)(xa[1]);
#pragma unroll
    for(int T=0;T<7;T++){ l0[T]=tr4(wRd[T]); h0[T]=tr4(wRd[T]+16*288); }
    l0[7]=tr4(wCs); h0[7]=tr4(wCs+16*288); }
  unsigned so=0;
  auto iter=[&](auto dA_c, auto dB_c, auto wv_c, auto sw_c){
    constexpr bool dA=decltype(dA_c)::value, dB=decltype(dB_c)::value, SW=decltype(sw_c)::value; constexpr int WV=decltype(wv_c)::value;
    if(dA) sw=(so>=2*P1_SLOT)?so-2*P1_SLOT:so+2*P1_SLOT;
    step(xl0,xh0,l0,h0,xl1,xh1,l1,h1,1,true,dA);
    const unsigned sn=(so==3*P1_SLOT)?0u:so+P1_SLOT; const unsigned d=sn-so;
#pragma unroll
    for(int T=0;T<7;T++) wRd[T]+=d;
    wCs+=d;
#pragma unroll
    for(int k=0;k<4;k++) xa[k]+=d;
    asm volatile("s_waitcnt vmcnt(%0)" :: "i"(WV) : "memory");
    LBAR();
    if(dB) sw=(so==0)?3*P1_SLOT:so-P1_SLOT;
    so=sn;
    if(SW) side_work();
    step(xl1,xh1,l1,h1,xl0,xh0,l0,h0,0,true,dB);
  };
  using T_=std::true_type; using F_=std::false_type; using W4=std::integral_constant<int,4>; using W0=std::integral_constant<int,0>;
  if(wid<4){
    for(int s=0;s<6;s++) iter(F_{},T_{},W4{},F_{});
    iter(F_{},F_{},W4{},T_{});
  } else {
    iter(F_{},F_{},W4{},F_{});
    for(int s=1;s<6;s++) iter(T_{},F_{},W4{},F_{});
    iter(T_{},F_{},W4{},T_{});
  }
  iter(F_{},F_{},W0{},F_{});
  step(xl0,xh0,l0,h0,xl1,xh1,l1,h1,1,true,false);
  step(xl1,xh1,l1,h1,xl0,xh0,l0,h0,0,false,false);
  STK(2);
  if(wid<7 && r16==0) *(LAS f32x4*)(lds+P1_CS+wid*64+g*16)=accs;
  const float sx=accx[0];
  const float dg=(r16&2)?((r16&1)?accq[3]:accq[2]):((r16&1)?accq[1]:accq[0]);
  const float sxx=__shfl(dg,r16+16*(r16>>2));
  const float mean=sx*(1.f/576.f); const float var=fmaxf(sxx*(1.f/576.f)-mean*mean,0.f); const float rstd=__builtin_amdgcn_rsqf(var+1e-5f);
  if(hp==0 && g==0) st4(wsr,WS_MEANX+(rt*128+16*wid+r16)*4,__float_as_uint(mean));
  LBAR();
  unsigned vb[7][2];
#pragma unroll
  for(int T=0;T<7;T++){ const f32x4 cs=*(LAS f32x4*)(lds+P1_CS+T*64+g*16); float v[4];
#pragma unroll
    for(int r=0;r<4;r++) v[r]=rstd*(acc[T][r]-mean*cs[r]);
    vb[T][0]=pk_bf16(v[0],v[1]); vb[T][1]=pk_bf16(v[2],v[3]); }
  auto wfr=[&](int fr)->s16x4{ return *(LAS s16x4*)(lds+P1_WF+fr*512+lane*8); };
  auto bq=[&](int T)->s16x4{ return __builtin_bit_cast(s16x4,(u32x2){vb[T][0],vb[T][1]}); };
  const f32x4 Z={0,0,0,0};
  f32x4 P[4], X;
  P[0]=MFMA16K16(wfr(0),bq(0),Z); P[0]=MFMA16K16(wfr(1),bq(1),P[0]);
  P[1]=MFMA16K16(wfr(2),bq(1),Z); P[1]=MFMA16K16(wfr(3),bq(2),P[1]);
  P[2]=MFMA16K16(wfr(4),bq(2),Z); P[2]=MFMA16K16(wfr(5),bq(3),P[2]);
  P[3]=MFMA16K16(wfr(6),bq(3),Z); P[3]=MFMA16K16(wfr(7),bq(4),P[3]);
  X=MFMA16K16(wfr(8),bq(0),Z); X=MFMA16K16(wfr(9),bq(1),X); X=MFMA16K16(wfr(10),bq(2),X); X=MFMA16K16(wfr(11),bq(3),X); X=MFMA16K16(wfr(12),bq(4),X);
  unsigned pos[2][2], neg[2][2]; float xpos[2], xneg[2];
  dual_softmax(P[2],X[2],g<2,pos[0],neg[0],xpos[0],xneg[0]);
  dual_softmax(P[3],X[3],g<2,pos[1],neg[1],xpos[1],xneg[1]);
  PreQ pq; pq.a=P[0]; pq.b=P[1]; pq.xa=X[0]; pq.xb=X[1];
  { LAS unsigned char* fr=lds+P1_F+(16*wid+r16)*160+g*8;
    *(LAS u32x2*)(fr)=(u32x2){pos[0][0],pos[0][1]}; *(LAS u32x2*)(fr+32)=(u32x2){neg[0][0],neg[0][1]};
    *(LAS u32x2*)(fr+64)=(u32x2){pos[1][0],pos[1][1]}; *(LAS u32x2*)(fr+96)=(u32x2){neg[1][0],neg[1][1]};
    *(LAS u32x2*)(fr+128)=(u32x2){pk_bf16(xpos[0],xneg[0]),pk_bf16(xpos[1],xneg[1])};
    LAS unsigned char* vr=lds+P1_V+(16*wid+r16)*96;
    if(g>=2) *(LAS u32x2*)(vr+(g-2)*8)=(u32x2){vb[4][0],vb[4][1]};
    *(LAS u32x2*)(vr+(2+g)*8)=(u32x2){vb[5][0],vb[5][1]};
    *(LAS u32x2*)(vr+(6+g)*8)=(g<3)?(u32x2){vb[6][0],vb[6][1]}:(u32x2){ONES,0u}; }
  LBAR();
  STK(3);
  if(wid<4){
    const unsigned fA=ldsb+P1_F+(4*g+q)*160+pp*8, vA=ldsb+P1_V+(4*g+q)*96+pp*8;
    auto ff=[&](int R,int ks){ return tr_frag(fA+(32*ks)*160+R*32, fA+(32*ks+16)*160+R*32); };
    auto vf=[&](int ct,int ks){ return tr_frag(vA+(32*ks)*96+ct*32, vA+(32*ks+16)*96+ct*32); };
    const unsigned kp=WS_KVP+(unsigned)(hp*16+rt)*4096u;
    auto pack2=[&](const f32x4& a,const f32x4& b){ return (u32x4){pk_bf16(a[0],a[1]),pk_bf16(a[2],a[3]),pk_bf16(b[0],b[1]),pk_bf16(b[2],b[3])}; };
    if(wid==0){ f32x4 a=Z,b=Z;
#pragma unroll
      for(int ks=0;ks<4;ks++){ const bf16x8 v=vf(0,ks); a=MFMA16(ff(0,ks),v,a); b=MFMA16(ff(1,ks),v,b); }
      st16(wsr,kp+lane*16,pack2(a,b));
    } else if(wid<3){ const int ct=wid; f32x4 pa=Z,pb=Z,na=Z,nb=Z;
#pragma unroll
      for(int ks=0;ks<4;ks++){ const bf16x8 v=vf(ct,ks); pa=MFMA16(ff(0,ks),v,pa); na=MFMA16(ff(1,ks),v,na); pb=MFMA16(ff(2,ks),v,pb); nb=MFMA16(ff(3,ks),v,nb); }
      const bool useA=(ct==1)?(r16<2):(r16==5);
      const f32x4 ps=useA?pa:pb, ns=useA?na:nb;
      if(ct==1 || r16<6) st16(wsr,kp+ct*1024+lane*16,pack2(ps,ns));
    } else { f32x4 e0=Z,e1=Z,e2=Z;
#pragma unroll
      for(int ks=0;ks<4;ks++){ const bf16x8 f=ff(4,ks); e0=MFMA16(f,vf(0,ks),e0); e1=MFMA16(f,vf(1,ks),e1); e2=MFMA16(f,vf(2,ks),e2); }
      if(lane<32){ st16(wsr,kp+3072+lane*16,pack2(e0,e1)); if(r16<6) st8(wsr,kp+3584+lane*8,(u32x2){pk_bf16(e2[0],e2[1]),pk_bf16(e2[2],e2[3])}); }
    }
  }
  return pq;
}

constexpr int P2B_B=0, P2B_A=83712, P2B_WM=P2B_A+4*18*1024, P2A_AF=P2B_A;
DI void prefetch_wo(const Params& p, unsigned ldsb, int cg){
  const int tid_=otid(); const int lane=tid_&63, wid=__builtin_amdgcn_readfirstlane(tid_>>6);
  if(wid>=4){ const int lr=lane/9, lc=lane%9; const char* wb=(const char*)p.wo+(size_t)cg*144;
    for(int id=wid-4;id<83;id+=4){ int row=7*id+lr; row=row>575?575:row; glds16((unsigned)(row*1152+lc*16),wb,ldsb+P2B_B+id*1008); } }
}
DI void phase2a(const Params& p, LAS unsigned char* lds, rsrc_t wsr, int rt, int hp, const PhiQ& ph){
  const int tid=otid(), lane=tid&63, wid=__builtin_amdgcn_readfirstlane(tid>>6);
  const int g=lane>>4, r16=lane&15;
  LAS unsigned char* af=lds+P2A_AF;
  const unsigned kp=WS_KVP+(unsigned)(hp*16)*4096u;
  if(wid<3){
    const bool act=(wid<2)||(r16<6);
    float s[8]={0,0,0,0,0,0,0,0};
    if(act){ u32x4 v[16];
#pragma unroll
      for(int i=0;i<16;i++) v[i]=ld16c(wsr,kp+i*4096+wid*1024+lane*16);
#pragma unroll
      for(int i=0;i<16;i++){
#pragma unroll
        for(int j=0;j<4;j++){ s[2*j]+=bflo(v[i][j]); s[2*j+1]+=bfhi(v[i][j]); } } }
    const u32x4 w={pk_bf16(s[0],s[1]),pk_bf16(s[2],s[3]),pk_bf16(s[4],s[5]),pk_bf16(s[6],s[7])}; const u32x4 z4={0,0,0,0};
    if(wid==0) *(LAS u32x4*)(af+lane*16)=w;
    else if(wid==1){ *(LAS u32x4*)(af+1024+lane*16)=(r16<2)?w:z4; *(LAS u32x4*)(af+2048+lane*16)=(r16<2)?z4:w; }
    else { *(LAS u32x4*)(af+3072+lane*16)=(r16==5)?w:z4; *(LAS u32x4*)(af+4096+lane*16)=(r16<5)?w:z4; }
  } else if(wid==3){
    float a[8]={0,0,0,0,0,0,0,0}, c[4]={0,0,0,0};
    if(lane<32){ u32x4 v[16]; u32x2 u[16];
#pragma unroll
      for(int i=0;i<16;i++){ v[i]=ld16c(wsr,kp+i*4096+3072+lane*16); u[i]=(r16<6)?ld8c(wsr,kp+i*4096+3584+lane*8):(u32x2){0u,0u}; }
#pragma unroll
      for(int i=0;i<16;i++){
#pragma unroll
        for(int j=0;j<4;j++){ a[2*j]+=bflo(v[i][j]); a[2*j+1]+=bfhi(v[i][j]); }
        c[0]+=bflo(u[i][0]); c[1]+=bfhi(u[i][0]); c[2]+=bflo(u[i][1]); c[3]+=bfhi(u[i][1]); } }
    const u32x2 E0={pk_bf16(a[0],a[1]),0u};
    const u32x2 E1=(r16<2)?(u32x2){pk_bf16(a[4],a[5]),0u}:(u32x2){0u,pk_bf16(a[6],a[7])};
    const u32x2 E2=(r16<5)?(u32x2){0u,pk_bf16(c[2],c[3])}:((r16==5)?(u32x2){pk_bf16(c[0],c[1]),0u}:(u32x2){0u,0u});
    *(LAS u32x2*)(af+5120+lane*8)=E0; *(LAS u32x2*)(af+5632+lane*8)=E1; *(LAS u32x2*)(af+6144+lane*8)=E2;
  }
  LBAR();
  auto AF=[&](int i){ return *(LAS bf16x8*)(af+i*1024+lane*16); };
  auto EF=[&](int i){ return *(LAS s16x4*)(af+5120+i*512+lane*8); };
  const bf16x8 fA=__builtin_bit_cast(bf16x8,ph.a), fB=__builtin_bit_cast(bf16x8,ph.b); const s16x4 fX=__builtin_bit_cast(s16x4,ph.x);
  const f32x4 Z={0,0,0,0};
  f32x4 D0=MFMA16(AF(0),fA,Z);
  f32x4 D1=MFMA16(AF(1),fA,Z); D1=MFMA16(AF(2),fB,D1);
  f32x4 D2=MFMA16(AF(3),fA,Z); D2=MFMA16(AF(4),fB,D2);
  D0+=MFMA16K16(EF(0),fX,Z); D1+=MFMA16K16(EF(1),fX,Z); D2+=MFMA16K16(EF(2),fX,Z);
  const float denB=__shfl(D2[0],16+r16), denA=__shfl(D2[1],16+r16);
  const float rA=__builtin_amdgcn_rcpf(denA+1e-6f), rB=__builtin_amdgcn_rcpf(denB+1e-6f);
  const float r10=(g==0)?rA:rB;
  const u32x4 w={pk_bf16(D0[0]*rA,D0[1]*rA),pk_bf16(D0[2]*rA,D0[3]*rA),pk_bf16(D1[0]*r10,D1[1]*r10),pk_bf16(D1[2]*rB,D1[3]*rB)};
  const unsigned ob=WS_O+(unsigned)(rt*8+wid)*18u*1024u;
  st16(wsr,ob+hp*1024+r16*64+((g^((r16>>2)&2))*16),w);
  if(g==0) st8(wsr,ob+(16+(hp>>3))*1024+r16*64+((((hp&7)>>1)^((r16>>2)&2))*16)+(hp&1)*8,(u32x2){pk_bf16(D2[0]*rB,D2[1]*rB),pk_bf16(D2[2]*rB,D2[3]*rB)});
}

DI void phase2b(const Params& p, LAS unsigned char* lds, rsrc_t wsr, int rt, int cg, const u32x2 (&xq)[3]){
  const int tid=otid(), lane=tid&63, wid=__builtin_amdgcn_readfirstlane(tid>>6);
  const int g=lane>>4, q=(lane>>2)&3, pp=lane&3, r16=lane&15;
  const int wm=wid&3, wn=wid>>2;
  const unsigned ldsb=(unsigned)(size_t)lds;
  const int token=rt*64+16*wm+r16;
  { const char* ob=p.ws+WS_O+(size_t)(rt*4)*18*1024;
#pragma unroll
    for(int k=0;k<9;k++) glds16c(lane*16,ob+(size_t)(wid*9+k)*1024,ldsb+P2B_A+(wid*9+k)*1024);
    if(wid==0){ glds16c((unsigned)(lane*16),p.ws+WS_WMF,ldsb+P2B_WM); glds16c((unsigned)((64+(lane<8?lane:7))*16),p.ws+WS_WMF,ldsb+P2B_WM+1024); } }
  const float mx=__uint_as_float(ld4c(wsr,WS_MEANX+token*4));
  WAIT_V(0); BAR();
  STK(8);
  const unsigned aRd=ldsb+P2B_A+wm*18*1024+r16*64+((g^((lane>>2)&2))*16);
  const unsigned tcol=(wn==0)?0u:64u;
  const unsigned bRd=ldsb+P2B_B+(4*g+q)*144+tcol+pp*8;
  const unsigned bRdC=ldsb+P2B_B+(36*(2*g)+32+q)*144+tcol+pp*8;
  const unsigned hoff=128u+(pp&1)*8u-tcol-pp*8u;
  const unsigned mRd=ldsb+P2B_WM+g*16;
  f32x4 a0={0,0,0,0},a1={0,0,0,0},a2={0,0,0,0},am={0,0,0,0};
#pragma unroll
  for(int k3=0;k3<6;k3++){
    bf16x8 of[3],w0[3],w1[3],w2[3],wmf[3];
#pragma unroll
    for(int j=0;j<3;j++){ const int kb=3*k3+j;
      const unsigned b0=(kb<16)?(bRd+kb*36*144):(bRdC+(kb-16)*8*36*144); const unsigned b1=b0+((kb<16)?16*144:36*144);
      of[j]=ld_frag(aRd+kb*1024); w0[j]=tr_frag(b0,b1); w1[j]=tr_frag(b0+32,b1+32); w2[j]=tr_frag(b0+hoff,b1+hoff); wmf[j]=ld_frag(mRd+kb*64); }
#pragma unroll
    for(int j=0;j<3;j++){ a0=MFMA16(w0[j],of[j],a0); a1=MFMA16(w1[j],of[j],a1); a2=MFMA16(w2[j],of[j],a2); am=MFMA16(wmf[j],of[j],am); }
  }
  const float mu=mx+am[0];
  float hv[12]; unsigned hb[6], cb[6]; float ss=0.f;
#pragma unroll
  for(int t=0;t<3;t++){ const f32x4 a=(t==0)?a0:((t==1)?a1:a2);
    const float xv[4]={bflo(xq[t][0]),bfhi(xq[t][0]),bflo(xq[t][1]),bfhi(xq[t][1])};
#pragma unroll
    for(int r=0;r<4;r++) hv[4*t+r]=xv[r]+rbf(a[r]);
    hb[2*t]=pk_bf16(hv[4*t],hv[4*t+1]); hb[2*t+1]=pk_bf16(hv[4*t+2],hv[4*t+3]);
    const float c0=bflo(hb[2*t])-mu, c1=bfhi(hb[2*t])-mu, c2=bflo(hb[2*t+1])-mu, c3=bfhi(hb[2*t+1])-mu;
    cb[2*t]=pk_bf16(c0,c1); cb[2*t+1]=pk_bf16(c2,c3);
    const bool on=(t<2)||(wn==1&&g<2);
    const float d0=bflo(cb[2*t]),d1=bfhi(cb[2*t]),d2=bflo(cb[2*t+1]),d3=bfhi(cb[2*t+1]);
    ss+=on?((d0*d0+d1*d1)+(d2*d2+d3*d3)):0.f; }
  ss=xr_sum(ss);
  const unsigned hr=WS_H+(unsigned)(token*576+72*cg+32*wn)*2u;
  st8(wsr,hr+8*g,(u32x2){hb[0],hb[1]}); st8(wsr,hr+32+8*g,(u32x2){hb[2],hb[3]});
  const unsigned hcb=WS_HC+(unsigned)(rt*4+wm)*18u*1024u;
  st16(wsr,hcb+(2*cg+wn)*1024+r16*64+((g^((r16>>2)&2))*16),(u32x4){cb[0],cb[1],cb[2],cb[3]});
  if(wn==1&&g<2){ st8(wsr,hr+64+8*g,(u32x2){hb[4],hb[5]});
    st8(wsr,hcb+(16+(cg>>2))*1024+r16*64+(((cg&3)^((r16>>2)&2))*16)+g*8,(u32x2){cb[4],cb[5]}); }
  if(g==0) st4(wsr,WS_SSQ+(unsigned)(token*16+2*cg+wn)*4u,__float_as_uint(ss));
}

constexpr int K3_SLOT=16384+18432, K3_ST=4*K3_SLOT;
struct P3W { unsigned voffB, voffB8, voffC, voffC8; };
DI unsigned p3_rowoff(int kb){ return (unsigned)((kb<16?(72*(kb>>1)+32*(kb&1)):(288*(kb-16)))*9216); }
DI P3W p3_wsetup(){ const int tid_=otid(); const int lane=tid_&63, wid=__builtin_amdgcn_readfirstlane(tid_>>6);
  const unsigned o0=wid*1024+lane*16, o8=8192+lane*16; const unsigned r0=o0/288, c0=o0%288, r8=o8/288, c8=o8%288;
  auto crow=[](unsigned rho){ return 72u*((rho>>2)&3u)+64u+4u*(rho>>4)+(rho&3u); };
  P3W w; w.voffB=r0*9216+c0; w.voffB8=r8*9216+c8; w.voffC=crow(r0)*9216+c0; w.voffC8=crow(r8)*9216+c8; return w; }
DI void prefetch_w13(const Params& p, unsigned ldsb, int nt, const P3W& w){
  const int wid=__builtin_amdgcn_readfirstlane(otid()>>6);
  const char* b1Base=(const char*)p.w1+(size_t)nt*288; const char* b3Base=(const char*)p.w3+(size_t)nt*288;
#pragma unroll
  for(int st=0;st<3;st++){ const unsigned sl=ldsb+st*K3_SLOT, ro=p3_rowoff(st);
    glds16(w.voffB+ro,b1Base,sl+16384+wid*1024); glds16(w.voffB+ro,b3Base,sl+16384+9216+wid*1024);
    if(wid<2) glds16(w.voffB8+ro,wid?b3Base:b1Base,sl+16384+wid*9216+8192); }
}
DI void phase3(const Params& p, LAS unsigned char* lds, rsrc_t wsr, int mt, int nt, const P3W& w){
  const int tid=otid(), lane=tid&63, wid=__builtin_amdgcn_readfirstlane(tid>>6);
  const int wm=wid&3, wn=wid>>2;
  const int m0=mt*256;
  const int g=lane>>4, q=(lane>>2)&3, pp=lane&3;
  const unsigned ldsb=(unsigned)(size_t)lds;
  const unsigned voffA=lane*16;
  const char* aBase=p.ws+WS_HC+(size_t)(mt*16+wid*2)*18*1024;
  const char* a1Base=aBase+18*1024;
  const char* b1Base=(const char*)p.w1+(size_t)nt*288;
  const char* b3Base=(const char*)p.w3+(size_t)nt*288;
  const char* bxBase=wid?b3Base:b1Base;
  const unsigned cB1=ldsb+16384+wid*1024, cB3=ldsb+16384+9216+wid*1024, cB8=ldsb+16384+wid*9216+8192, cA=ldsb+wid*2048;
  { const char* sb=p.ws+WS_SSQ+(size_t)(m0+32*wid)*64; glds16c(voffA,sb,ldsb+K3_ST+wid*2048); glds16c(voffA,sb+1024,ldsb+K3_ST+wid*2048+1024); }
#pragma unroll
  for(int st=0;st<3;st++){ glds16c(voffA+st*1024,aBase,st*K3_SLOT+cA); glds16c(voffA+st*1024,a1Base,st*K3_SLOT+cA+1024); }
  const unsigned aRd=ldsb+(4*wm)*1024+(lane&15)*64+((g^((lane>>2)&2))*16);
  const unsigned bRd=ldsb+16384+(4*g+q)*288+(4*wn+(nt&1))*32+pp*8;
  const unsigned bRdM=ldsb+16384+(pp>>1)*9216+(4*g+q)*288+((nt&1)?0:256)+wn*16+(pp&1)*8;
  f32x4 ag[4][4], au[4][4], am[4];
#pragma unroll
  for(int mi=0;mi<4;mi++){ am[mi]=(f32x4){0,0,0,0};
#pragma unroll
    for(int i=0;i<4;i++){ ag[mi][i]=(f32x4){0,0,0,0}; au[mi][i]=(f32x4){0,0,0,0}; } }
  unsigned vB=w.voffB+p3_rowoff(3), vB8=w.voffB8+p3_rowoff(3), vA=voffA+3*1024;
  unsigned rA=aRd, rB=bRd, rM=bRdM;
  unsigned sRd=0, sWr=3*K3_SLOT;
  int nstep=3;
  WAIT_V(4);
  BAR();
  STK(11);
  if(wn==1) BAR();
  auto body=[&](auto dma_c, auto wait_c){
    constexpr bool DMA=decltype(dma_c)::value; constexpr int WV=decltype(wait_c)::value;
    bf16x8 xa[4], wg[4], wu[4], wx;
#pragma unroll
    for(int mi=0;mi<4;mi++) xa[mi]=ld_frag(rA+mi*1024);
#pragma unroll
    for(int t=0;t<4;t++){ wg[t]=tr_frag(rB+t*32,rB+16*288+t*32); wu[t]=tr_frag(rB+9216+t*32,rB+9216+16*288+t*32); }
    wx=tr_frag(rM,rM+16*288);
    SB0();
    if(DMA){
      asm volatile("s_add_u32 m0, %0, %1\n\ts_nop 0\n\tglobal_load_lds_dwordx4 %5, %7\n\t"
                   "s_add_u32 m0, %0, %2\n\ts_nop 0\n\tglobal_load_lds_dwordx4 %5, %8\n\t"
                   "s_add_u32 m0, %0, %3\n\ts_nop 0\n\tglobal_load_lds_dwordx4 %6, %9 sc1\n\t"
                   "s_add_u32 m0, %0, %4\n\ts_nop 0\n\tglobal_load_lds_dwordx4 %6, %10 sc1"
                   :: "s"(sWr),"s"(cB1),"s"(cB3),"s"(cA),"s"(cA+1024),"v"(vB),"v"(vA),"s"(b1Base),"s"(b3Base),"s"(aBase),"s"(a1Base) : "memory","scc");
      if(wid<2) asm volatile("s_add_u32 m0, %0, %1\n\ts_nop 0\n\tglobal_load_lds_dwordx4 %2, %3" :: "s"(sWr),"s"(cB8),"v"(vB8),"s"(bxBase) : "memory","scc");
    }
    asm volatile("s_waitcnt vmcnt(%0)" :: "i"(WV) : "memory");
    WAIT_L(0); BAR();
    __builtin_amdgcn_s_setprio(1);
#pragma unroll
    for(int mi=0;mi<4;mi++){
#pragma unroll
      for(int t=0;t<4;t++){ ag[mi][t]=MFMA16(wg[t],xa[mi],ag[mi][t]); au[mi][t]=MFMA16(wu[t],xa[mi],au[mi][t]); }
      am[mi]=MFMA16(wx,xa[mi],am[mi]);
    }
    sWr=sRd; { unsigned n=sRd+K3_SLOT; n=(n==4*K3_SLOT)?0u:n; const unsigned d=n-sRd; sRd=n; rA+=d; rB+=d; rM+=d; }
    if(DMA){
      const int ns=nstep+1; nstep=ns;
      if(ns==16){ vB=w.voffC; vB8=w.voffC8; } else { const unsigned d=(ns>16)?288u*9216u:((ns&1)?32u*9216u:40u*9216u); vB+=d; vB8+=d; }
      vA+=1024; }
    asm volatile("" : "+v"(rA),"+v"(rB),"+v"(rM),"+v"(vB),"+v"(vB8),"+v"(vA));
    __builtin_amdgcn_s_setprio(0);
    BAR();
  };
  using T=std::true_type; using Fz=std::false_type;
  body(T{},std::integral_constant<int,6>{});
  for(int i=1;i<15;i++) body(T{},std::integral_constant<int,8>{});
  body(Fz{},std::integral_constant<int,4>{});
  body(Fz{},std::integral_constant<int,0>{});
  body(Fz{},std::integral_constant<int,0>{});
  if(wn==0) BAR();
  STK(12);
  float cc[4], nl[4];
#pragma unroll
  for(int mi=0;mi<4;mi++){ const f32x4 sp=*(LAS f32x4*)(lds+K3_ST+(64*wm+16*mi+(lane&15))*64+g*16); float v=(sp[0]+sp[1])+(sp[2]+sp[3]); v=xr_sum(v); v=v*(1.f/576.f)+1e-5f; cc[mi]=v; nl[mi]=-LOG2E*__builtin_amdgcn_rsqf(v); }
  auto act=[&](float a,float u,int mi)->float{ float e=__builtin_amdgcn_exp2f(a*nl[mi]); float d=__builtin_fmaf(e,cc[mi],cc[mi]); return a*u*__builtin_amdgcn_rcpf(d); };
  const int kb0=9*(nt>>1)+5*(nt&1)+2*wn;
  const unsigned lo=(lane&15)*64+((g^((lane>>2)&2))*16);
#pragma unroll
  for(int mi=0;mi<4;mi++){
    const unsigned bp=WS_HM+((unsigned)(mt*16+4*wm+mi)*144u+kb0)*1024u+lo;
#pragma unroll
    for(int tp=0;tp<2;tp++){
      const f32x4 G0=ag[mi][2*tp], U0=au[mi][2*tp], G1=ag[mi][2*tp+1], U1=au[mi][2*tp+1];
      u32x4 v; v[0]=pk_bf16(act(G0[0],U0[0],mi),act(G0[1],U0[1],mi)); v[1]=pk_bf16(act(G0[2],U0[2],mi),act(G0[3],U0[3],mi));
      v[2]=pk_bf16(act(G1[0],U1[0],mi),act(G1[1],U1[1],mi)); v[3]=pk_bf16(act(G1[2],U1[2],mi),act(G1[3],U1[3],mi));
      st16(wsr,bp+tp*1024,v);
    }
  }
#pragma unroll
  for(int mp=0;mp<2;mp++){
    float G[4],U[4];
#pragma unroll
    for(int r=0;r<4;r++){ auto s=__builtin_amdgcn_permlane32_swap(__float_as_uint(am[2*mp][r]),__float_as_uint(am[2*mp+1][r]),false,false); G[r]=__uint_as_float(s[0]); U[r]=__uint_as_float(s[1]); }
    const int mi=2*mp+(lane>>5);
    const float ccx=(lane>>5)?cc[2*mp+1]:cc[2*mp], nlx=(lane>>5)?nl[2*mp+1]:nl[2*mp];
    float o[4];
#pragma unroll
    for(int r=0;r<4;r++){ float e=__builtin_amdgcn_exp2f(G[r]*nlx); float d=__builtin_fmaf(e,ccx,ccx); o[r]=G[r]*U[r]*__builtin_amdgcn_rcpf(d); }
    const int c=2*wn+(g&1);
    st8(wsr,WS_HM+((unsigned)(mt*16+4*wm+mi)*144u+9*(nt>>1)+4)*1024u+(lane&15)*64+((c^((lane>>2)&2))*16)+(nt&1)*8,(u32x2){pk_bf16(o[0],o[1]),pk_bf16(o[2],o[3])});
  }
}

constexpr int K4_SLOT=32768+18432;
struct P4W { unsigned v0,v1,v2; };
DI P4W p4_wsetup(){ const int tid_=otid(); const int lane=tid_&63, wid=__builtin_amdgcn_readfirstlane(tid_>>6);
  P4W w; const unsigned o0=wid*1024+lane*16, o1=o0+8192, o2=16384+(wid&1)*1024+lane*16;
  w.v0=(o0/288)*1152+o0%288; w.v1=(o1/288)*1152+o1%288; w.v2=(o2/288)*1152+o2%288; return w; }
DI void p4_issue_w(const Params& p, unsigned slot, int sl_, int ct, int st, const P4W& w){
  const int wid=__builtin_amdgcn_readfirstlane(otid()>>6);
  const char* wb=(const char*)p.w2+((size_t)(sl_*576+st*64)*576+ct*144)*2;
  glds16(w.v0,wb,slot+32768+wid*1024); glds16(w.v1,wb,slot+32768+8192+wid*1024);
  if(wid<2) glds16(w.v2,wb,slot+32768+16384+wid*1024); }
DI void prefetch_w2(const Params& p, unsigned ldsb, int sl_, int ct, const P4W& w){
#pragma unroll
  for(int st=0;st<3;st++) p4_issue_w(p,ldsb+st*K4_SLOT,sl_,ct,st,w); }
template<int NT> DI void p4_loop(const Params& p, unsigned ldsb, int sl_, int ct, const P4W& w, const char* aBase, int lane, int wid, f32x4 (&acc)[4][5]){
  const int wm=wid&3; const int g=lane>>4, q=(lane>>2)&3, pp=lane&3;
  const unsigned aRd=ldsb+(8*wm)*1024+(lane&15)*64+((g^((lane>>2)&2))*16);
  const unsigned bRd=ldsb+32768+(4*g+q)*288+(NT==4?160:0)+pp*8;
  const unsigned cW0=ldsb+32768+wid*1024, cW1=cW0+8192, cW2=cW0+16384, cA=ldsb+wid*4096;
  bf16x8 x0[4], x1[4]; s16x4 l0[NT], h0[NT], l1[NT], h1[NT];
  const char* wb=(const char*)p.w2+((size_t)(sl_*576+3*64)*576+ct*144)*2;
  const char* ab=aBase+3*2048; const unsigned va=lane*16;
  unsigned sw=0;
  auto dma=[&](int k){
    if(k==0) asm volatile("s_add_u32 m0, %0, %1\n\ts_nop 0\n\tglobal_load_lds_dwordx4 %2, %3" :: "s"(sw),"s"(cW0),"v"(w.v0),"s"(wb) : "memory","scc");
    if(k==1) asm volatile("s_add_u32 m0, %0, %1\n\ts_nop 0\n\tglobal_load_lds_dwordx4 %2, %3" :: "s"(sw),"s"(cW1),"v"(w.v1),"s"(wb) : "memory","scc");
    if(k==2){ if(wid<2) asm volatile("s_add_u32 m0, %0, %1\n\ts_nop 0\n\tglobal_load_lds_dwordx4 %2, %3" :: "s"(sw),"s"(cW2),"v"(w.v2),"s"(wb) : "memory","scc"); }
    if(k==3) asm volatile("s_add_u32 m0, %0, %1\n\ts_nop 0\n\tglobal_load_lds_dwordx4 %2, %3 sc1" :: "s"(sw),"s"(cA),"v"(va),"s"(ab) : "memory","scc");
    if(k==4) asm volatile("s_add_u32 m0, %0, %1\n\ts_nop 0\n\tglobal_load_lds_dwordx4 %2, %3 sc1" :: "s"(sw),"s"(cA+1024),"v"(va),"s"(ab+1024) : "memory","scc");
    if(k==5) asm volatile("s_add_u32 m0, %0, %1\n\ts_nop 0\n\tglobal_load_lds_dwordx4 %2, %3 sc1" :: "s"(sw),"s"(cA+2048),"v"(va),"s"(ab+144*1024) : "memory","scc");
    if(k==6) asm volatile("s_add_u32 m0, %0, %1\n\ts_nop 0\n\tglobal_load_lds_dwordx4 %2, %3 sc1" :: "s"(sw),"s"(cA+3072),"v"(va),"s"(ab+145*1024) : "memory","scc");
  };
  auto step=[&](bf16x8 (&X)[4], s16x4 (&L)[NT], s16x4 (&Hh)[NT], bf16x8 (&XN)[4], s16x4 (&LN)[NT], s16x4 (&HN)[NT], unsigned so, int kk, bool rd, bool dm){
    const unsigned ra=aRd+so+kk*1024, rb=bRd+so+(32*kk)*288;
#pragma unroll
    for(int c=0;c<NT;c++){
      if(rd){
#pragma unroll
        for(int j=3*c;j<3*c+3;j++){
          if(j<4) XN[j]=ld_frag(ra+j*2048);
          else if(j<4+2*NT){ const int t=(j-4)>>1; if(((j-4)&1)==0) LN[t]=tr4(rb+t*32); else HN[t]=tr4(rb+16*288+t*32); }
        }
      }
      { const bf16x8 wf=cat8(L[c],Hh[c]);
#pragma unroll
        for(int mi=0;mi<4;mi++) acc[mi][c]=MFMA16(wf,X[mi],acc[mi][c]); }
      __builtin_amdgcn_sched_group_barrier(0x008,1,0); __builtin_amdgcn_sched_group_barrier(0x100,1,0);
      __builtin_amdgcn_sched_group_barrier(0x008,1,0); __builtin_amdgcn_sched_group_barrier(0x100,1,0);
      __builtin_amdgcn_sched_group_barrier(0x008,1,0); __builtin_amdgcn_sched_group_barrier(0x100,1,0);
      __builtin_amdgcn_sched_group_barrier(0x008,1,0);
      if(dm){ SB0(); if(NT==5){ dma(c); if(c<2) dma(5+c); } else { dma(c); if(c<3) dma(4+c); } SB0(); }
    }
    if(dm){ wb+=64*1152; ab+=2048; }
  };
  { const unsigned ra=aRd, rb=bRd;
#pragma unroll
    for(int mi=0;mi<4;mi++) x0[mi]=ld_frag(ra+mi*2048);
#pragma unroll
    for(int t=0;t<NT;t++){ l0[t]=tr4(rb+t*32); h0[t]=tr4(rb+16*288+t*32); } }
  unsigned so=0;
  auto iter=[&](auto dA_c, auto dB_c, auto wv_c){
    constexpr bool dA=decltype(dA_c)::value, dB=decltype(dB_c)::value; constexpr int WV=decltype(wv_c)::value;
    const unsigned sn=(so==2*K4_SLOT)?0u:so+K4_SLOT;
    step(x0,l0,h0,x1,l1,h1,so,1,true,dA);
    asm volatile("s_waitcnt vmcnt(%0)" :: "i"(WV) : "memory");
    LBAR();
    sw=so;
    step(x1,l1,h1,x0,l0,h0,sn,0,true,dB);
    so=sn;
  };
  using T=std::true_type; using Fz=std::false_type; using W6=std::integral_constant<int,6>; using W0=std::integral_constant<int,0>;
  using W4=std::integral_constant<int,4>;
  if(NT==5){
    iter(Fz{},T{},W4{});
    for(int st=1;st<6;st++) iter(Fz{},T{},W6{});
    iter(Fz{},Fz{},W6{}); iter(Fz{},Fz{},W0{});
  } else {
    iter(Fz{},Fz{},W4{});
    for(int st=1;st<7;st++) iter(T{},Fz{},W6{});
    iter(Fz{},Fz{},W0{});
  }
  step(x0,l0,h0,x1,l1,h1,so,1,true,false);
  step(x1,l1,h1,x0,l0,h0,so,0,false,false);
}
DI void phase4(const Params& p, LAS unsigned char* lds, rsrc_t wsr, int mt, int ct, int sl_, const P4W& w){
  const int tid=otid(), lane=tid&63, wid=__builtin_amdgcn_readfirstlane(tid>>6);
  const int wm=wid&3, wn=wid>>2;
  const unsigned ldsb=(unsigned)(size_t)lds;
  const unsigned voffA=lane*16;
  const char* aBase=p.ws+WS_HM+((size_t)(mt*16+wid*2)*144+18*sl_)*1024;
#pragma unroll
  for(int st=0;st<3;st++){ const char* ab=aBase+(size_t)(2*st)*1024; const unsigned sl=ldsb+st*K4_SLOT;
    glds16c(voffA,ab,sl+(wid*4)*1024); glds16c(voffA,ab+1024,sl+(wid*4+1)*1024);
    glds16c(voffA,ab+144*1024,sl+(wid*4+2)*1024); glds16c(voffA,ab+145*1024,sl+(wid*4+3)*1024); }
  f32x4 acc[4][5];
#pragma unroll
  for(int mi=0;mi<4;mi++)
#pragma unroll
    for(int t=0;t<5;t++) acc[mi][t]=(f32x4){0,0,0,0};
  WAIT_V(8); BAR();
  STK(15);
  if(wn==0) p4_loop<5>(p,ldsb,sl_,ct,w,aBase,lane,wid,acc); else p4_loop<4>(p,ldsb,sl_,ct,w,aBase,lane,wid,acc);
  STK(16);
#pragma unroll
  for(int mi=0;mi<4;mi++){
    const unsigned bp=WS_PART+(((unsigned)sl_*128u+mt*16+4*wm+mi)*4u+ct)*4608u+(wn?2560:0);
#pragma unroll
    for(int tp=0;tp<2;tp++){ const f32x4 a=acc[mi][2*tp], b=acc[mi][2*tp+1];
      st16(wsr,bp+tp*1024+lane*16,(u32x4){pk_bf16(a[0],a[1]),pk_bf16(a[2],a[3]),pk_bf16(b[0],b[1]),pk_bf16(b[2],b[3])}); }
    if(wn==0){ const f32x4 a=acc[mi][4]; st8(wsr,bp+2048+lane*8,(u32x2){pk_bf16(a[0],a[1]),pk_bf16(a[2],a[3])}); }
  }
}

struct P5C { u32x4 v[8]; u32x2 hA,hB; int rowA,colA,rowB,colB; };
DI void p5_load(const Params& p, rsrc_t wsr, int unit, int ch, P5C& c){
  const int mb=unit>>2, ct=unit&3;
  const unsigned src=WS_PART+(unsigned)unit*4608u+ch*16;
#pragma unroll
  for(int s=0;s<8;s++) c.v[s]=ld16c(wsr,src+s*(128u*4*4608));
  const int bo=ch*16; int tA,tB,lnA,lnB;
  if(bo<2048){ tA=2*(bo>>10); tB=tA+1; lnA=lnB=(bo&1023)>>4; }
  else if(bo<2560){ tA=tB=4; lnA=(bo-2048)>>3; lnB=lnA+1; }
  else { const int b2=bo-2560; tA=5+2*(b2>>10); tB=tA+1; lnA=lnB=(b2&1023)>>4; }
  c.rowA=mb*16+(lnA&15); c.colA=ct*144+16*tA+4*(lnA>>4); c.rowB=mb*16+(lnB&15); c.colB=ct*144+16*tB+4*(lnB>>4);
  c.hA=ld8c(wsr,WS_H+(unsigned)(c.rowA*576+c.colA)*2u); c.hB=ld8c(wsr,WS_H+(unsigned)(c.rowB*576+c.colB)*2u); }
DI void p5_fin(const Params& p, const P5C& c){
  float s[8]={0,0,0,0,0,0,0,0};
#pragma unroll
  for(int i=0;i<8;i++){
#pragma unroll
    for(int j=0;j<4;j++){ s[2*j]+=bflo(c.v[i][j]); s[2*j+1]+=bfhi(c.v[i][j]); } }
  const u32x2 oA={pk_bf16(bflo(c.hA[0])+rbf(s[0]),bfhi(c.hA[0])+rbf(s[1])),pk_bf16(bflo(c.hA[1])+rbf(s[2]),bfhi(c.hA[1])+rbf(s[3]))};
  const u32x2 oB={pk_bf16(bflo(c.hB[0])+rbf(s[4]),bfhi(c.hB[0])+rbf(s[5])),pk_bf16(bflo(c.hB[1])+rbf(s[6]),bfhi(c.hB[1])+rbf(s[7]))};
  *(u32x2*)(p.out+(size_t)c.rowA*576+c.colA)=oA; *(u32x2*)(p.out+(size_t)c.rowB*576+c.colB)=oB; }
DI void phase5(const Params& p, rsrc_t wsr, int mb0, int ct){
  const int tid_=otid(); const int lane=tid_&63, wid=tid_>>6;
  P5C c0, c1;
  const int k0=wid*72+lane, k1=wid*72+64+(lane&7);
  p5_load(p,wsr,(mb0+(k0>=288))*4+ct,k0>=288?k0-288:k0,c0);
  p5_load(p,wsr,(mb0+(k1>=288))*4+ct,k1>=288?k1-288:k1,c1);
  p5_fin(p,c0);
  if(lane<8) p5_fin(p,c1);
}

constexpr int MK_LDS=163840;
__global__ __launch_bounds__(512,2) void fused_kernel(Params p){
  extern __shared__ __attribute__((aligned(16))) unsigned char shm[];
  LAS unsigned char* lds=(LAS unsigned char*)shm;
  const unsigned ldsb=(unsigned)(size_t)lds;
  const int bid=blockIdx.x, xcd=bid&7, idx=bid>>3;
  STK(0);
  const rsrc_t wsr=__builtin_amdgcn_make_buffer_rsrc((void*)p.ws,0,WS_END,0x00020000);
  const u64 epoch=flag_ld(p.ws,FL_EPOCH);
  const int rt1=(xcd>>1)*4+(idx&3), hp1=(xcd&1)*8+(idx>>2);
  const PreQ pq=phase1(p,lds,wsr,rt1,hp1);
  STK(4);
  u64 tag; { const u64 e=epoch+1; tag=((u64)(unsigned)__builtin_amdgcn_readfirstlane((unsigned)(e>>32))<<32)|(unsigned)__builtin_amdgcn_readfirstlane((unsigned)e); }
  publish(p.ws,FL_1+(hp1*16+rt1)*8,tag,0);
  const int rt2=xcd*4+(idx&3), cg2=idx>>2;
  prefetch_wo(p,ldsb,cg2);
  u32x2 xq[3];
  { const int tid=otid(), lane=tid&63, wid=tid>>6; const int wm=wid&3, wn=wid>>2, g=lane>>4; const u16* xr=p.x+(size_t)(rt2*64+16*wm+(lane&15))*576+72*cg2+32*wn;
    xq[0]=*(const u32x2*)(xr+4*g); xq[1]=*(const u32x2*)(xr+16+4*g); xq[2]=(wn==1)?*(const u32x2*)(xr+32+4*(g&1)):(u32x2){0u,0u}; }
  const PhiQ ph=finish_q(pq);
  if(otid()<256) poll_flags(p.ws,FL_1+hp1*128,16,tag);
  STK(34); STK(35); STK(5);
  phase2a(p,lds,wsr,rt1,hp1,ph);
  STK(6);
  publish(p.ws,FL_2+(rt1*16+hp1)*8,tag,1);
  wait_flags(p.ws,FL_2+(rt2>>1)*128,16,tag,1);
  STK(7);
  phase2b(p,lds,wsr,rt2,cg2,xq);
  STK(9);
  publish(p.ws,FL_3+(rt2>>2)*256+((rt2&3)*8+cg2)*8,tag,2);
  const int mt3=idx>>2, nt3=xcd*4+(idx&3);
  const P3W w3=p3_wsetup();
  prefetch_w13(p,ldsb,nt3,w3);
  wait_flags(p.ws,FL_3+mt3*256,32,tag,2);
  STK(10);
  phase3(p,lds,wsr,mt3,nt3,w3);
  STK(13);
  publish(p.ws,FL_4+(mt3*8+xcd)*128+(idx&3)*8,tag,3);
  const P4W w4=p4_wsetup();
  prefetch_w2(p,ldsb,xcd,idx&3,w4);
  wait_flags(p.ws,FL_4+(mt3*8+xcd)*128,4,tag,3);
  STK(14);
  phase4(p,lds,wsr,mt3,idx&3,xcd,w4);
  STK(17);
  publish(p.ws,FL_5+(mt3*4+(idx&3))*128+xcd*8,tag,4);
  const int mt5=bid>>5, ct5=(bid>>3)&3, part5=bid&7;
  wait_flags(p.ws,FL_5+(mt5*4+ct5)*128,8,tag,4);
  STK(18);
  phase5(p,wsr,mt5*16+2*part5,ct5);
  STK(19);
  if(bid==0 && otid()==0) flag_st(p.ws,FL_EPOCH,tag);
}
extern "C" void kernel_launch(void* const* d_in, const int* in_sizes, int n_in, void* d_out, int out_size, void* d_ws, size_t ws_size, hipStream_t stream) {
  Params p; p.x=(const u16*)d_in[0]; p.wqkv=(const u16*)d_in[1]; p.wphi=(const u16*)d_in[2]; p.wo=(const u16*)d_in[3];
  p.w1=(const u16*)d_in[5]; p.w3=(const u16*)d_in[6]; p.w2=(const u16*)d_in[7]; p.out=(u16*)d_out; p.ws=(char*)d_ws;
  (void)hipFuncSetAttribute((const void*)fused_kernel,hipFuncAttributeMaxDynamicSharedMemorySize,MK_LDS);
  fused_kernel<<<256,512,MK_LDS,stream>>>(p);
}
```
